```python
import torch

def _softmax(x, axis=-1):
    return torch.softmax(x, dim=axis)
def _gelu(x):
    return torch.nn.functional.gelu(x, approximate="tanh")
def _silu(x):
    return torch.nn.functional.silu(x)
def _sigmoid(x):
    return torch.sigmoid(x)
def _layernorm(x, axis=-1, eps=1e-5):
    m = x.mean(dim=axis, keepdim=True)
    v = x.var(dim=axis, keepdim=True, unbiased=False)
    return (x - m) / torch.sqrt(v + eps)
def _scatter_add(tgt, idx, src, axis=0):
    return tgt.clone().index_add_(axis, idx, src)
def _segment_sum(src, seg, n):
    out = torch.zeros((n,) + src.shape[1:], dtype=src.dtype)
    return out.index_add_(0, seg, src)
def _pad(x, pw):
    flat = tuple(v for pair in reversed(pw) for v in pair)
    return torch.nn.functional.pad(x, flat)

kernel_name = 'standalone__deepnorm__sampling'

def setup_inputs():

    _w = (lambda a: (lambda t: t.to(torch.bfloat16) if t.is_floating_point() else t)(torch.as_tensor(a)))
    B, S, D = 1024, 8, 512
    return {
        "x": _w((lambda *s: torch.randn(*s, dtype=torch.bfloat16) * 0.05)(B, S, D)),
        "f": _w((lambda *s: torch.randn(*s, dtype=torch.bfloat16) * 0.05)(B, S, D)),
        "g": _w((lambda *s: torch.randn(*s, dtype=torch.bfloat16) * 0.05)(D) + 1.0),
        "b": _w((lambda *s: torch.randn(*s, dtype=torch.bfloat16) * 0.4)(D)),
        "alpha": _w(float(2.0)),
    }

def reference(x, f, g, b, alpha):
    return _layernorm(alpha * x + f, axis=-1) * g + b
```

```cpp
#include <hip/hip_runtime.h>
#include <hip/hip_bf16.h>
#include <stdint.h>

typedef __attribute__((ext_vector_type(4))) unsigned int u32x4;
typedef __attribute__((ext_vector_type(2))) float f32x2;
typedef __attribute__((ext_vector_type(2))) __bf16 bf16x2;

__device__ __forceinline__ f32x2 unpack2(unsigned int u, unsigned int hi_mask) {
  f32x2 r = {__uint_as_float(u << 16), __uint_as_float(u & hi_mask)};
  return r;
}
__device__ __forceinline__ unsigned int pack2(f32x2 v) {
  return __builtin_bit_cast(unsigned int, __builtin_convertvector(v, bf16x2));
}
__device__ __forceinline__ f32x2 fma2(f32x2 a, f32x2 b, f32x2 c) { return __builtin_elementwise_fma(a, b, c); }

template <int CTRL>
__device__ __forceinline__ float dpp_add(float v) {
  int t = __builtin_amdgcn_update_dpp(0, __float_as_int(v), CTRL, 0xf, 0xf, true);
  return v + __int_as_float(t);
}

__device__ __forceinline__ float wave_sum(float v) {
  v = dpp_add<0xB1>(v);
  v = dpp_add<0x4E>(v);
  v = dpp_add<0x141>(v);
  v = dpp_add<0x140>(v);
  v = dpp_add<0x142>(v);
  v = dpp_add<0x143>(v);
  return __int_as_float(__builtin_amdgcn_readlane(__float_as_int(v), 63));
}

__device__ __forceinline__ u32x4 norm_row(const u32x4& xv, const u32x4& fv, const f32x2* G, const f32x2* B, f32x2 al,
                                          unsigned int mask) {
  f32x2 h[4];
#pragma unroll
  for (int j = 0; j < 4; ++j) h[j] = fma2(al, unpack2(xv[j], mask), unpack2(fv[j], mask));
  const f32x2 s = (h[0] + h[1]) + (h[2] + h[3]);
  const float S = wave_sum(s[0] + s[1]);
  const f32x2 SS = {S, S}, nc = {-1.0f / 512.0f, -1.0f / 512.0f};
#pragma unroll
  for (int j = 0; j < 4; ++j) h[j] = fma2(SS, nc, h[j]);
  const f32x2 q = fma2(h[2], h[2], h[0] * h[0]) + fma2(h[3], h[3], h[1] * h[1]);
  f32x2 w[4];
#pragma unroll
  for (int j = 0; j < 4; ++j) w[j] = h[j] * G[j];
  const float r = __builtin_amdgcn_rsqf(__builtin_fmaf(wave_sum(q[0] + q[1]), 1.0f / 512.0f, 1e-5f));
  const f32x2 rr = {r, r};
  u32x4 o;
#pragma unroll
  for (int j = 0; j < 4; ++j) o[j] = pack2(fma2(rr, w[j], B[j]));
  return o;
}

#define NT_LOAD(p) __builtin_nontemporal_load((const u32x4*)(p))

__global__ __attribute__((aligned(2048))) __launch_bounds__(256) void deepnorm512_kernel(
    const char* __restrict__ x, const char* __restrict__ f, unsigned long long grp, const char* __restrict__ g,
    const char* __restrict__ b, const char* __restrict__ alpha_p, char* __restrict__ out) {
  const unsigned int bid = blockIdx.x;
  const unsigned int sbase = (bid >> 1) * (unsigned int)grp + (bid & 1u) * 4096u + ((bid >> 4) & 1u) * 8192u;
  const unsigned int offA = sbase + threadIdx.x * 16u;
  const unsigned int offB = offA ^ 8192u;
  u32x4 xa = NT_LOAD(x + offA);
  u32x4 fa = NT_LOAD(f + offA);
  __builtin_amdgcn_sched_barrier(0);
  const unsigned int lane16 = (threadIdx.x & 63) * 16u;
  const u32x4 gv = *(const u32x4*)(g + lane16);
  const u32x4 bv = *(const u32x4*)(b + lane16);
  __builtin_amdgcn_sched_barrier(0);
  const unsigned int aw =
      *(const __attribute__((address_space(4))) unsigned int*)((uintptr_t)alpha_p & ~(uintptr_t)3);
  const float alpha = __uint_as_float(((uintptr_t)alpha_p & 2) ? (aw & 0xffff0000u) : (aw << 16));
  const f32x2 al = {alpha, alpha};
  unsigned int mask;
  asm("s_mov_b32 %0, 0xffff0000" : "=s"(mask));
  __amdgpu_buffer_rsrc_t ro = __builtin_amdgcn_make_buffer_rsrc(out, 0, 0xffffffffu, 0x00020000);

  __builtin_amdgcn_sched_barrier(0);
  asm volatile("" : "+v"(xa));
  __builtin_amdgcn_sched_barrier(0);
  const u32x4 xb = NT_LOAD(x + offB);
  __builtin_amdgcn_sched_barrier(0);
  asm volatile("" : "+v"(fa));
  __builtin_amdgcn_sched_barrier(0);
  const u32x4 fb = NT_LOAD(f + offB);
  __builtin_amdgcn_sched_barrier(0);

  f32x2 G[4], B[4];
#pragma unroll
  for (int j = 0; j < 4; ++j) {
    G[j] = unpack2(gv[j], mask);
    B[j] = unpack2(bv[j], mask);
  }
  const u32x4 oa = norm_row(xa, fa, G, B, al, mask);
  __builtin_amdgcn_raw_buffer_store_b128(oa, ro, offA, 0,   16);
  __builtin_amdgcn_sched_barrier(0);
  const u32x4 ob = norm_row(xb, fb, G, B, al, mask);
  __builtin_amdgcn_raw_buffer_store_b128(ob, ro, offB, 0, 0);
}

__global__ __launch_bounds__(64) void deepnorm_generic_kernel(const __hip_bfloat16* __restrict__ x,
                                                              const __hip_bfloat16* __restrict__ f,
                                                              const __hip_bfloat16* __restrict__ g,
                                                              const __hip_bfloat16* __restrict__ b,
                                                              const __hip_bfloat16* __restrict__ alpha_p,
                                                              __hip_bfloat16* __restrict__ out, long long D) {
  const long long base = (long long)blockIdx.x * D;
  const float alpha = __bfloat162float(alpha_p[0]);
  float s = 0.f;
  for (long long i = threadIdx.x; i < D; i += 64)
    s += __builtin_fmaf(alpha, __bfloat162float(x[base + i]), __bfloat162float(f[base + i]));
  for (int o = 32; o > 0; o >>= 1) s += __shfl_xor(s, o, 64);
  const float mean = s / (float)D;
  float q = 0.f;
  for (long long i = threadIdx.x; i < D; i += 64) {
    const float d = __builtin_fmaf(alpha, __bfloat162float(x[base + i]), __bfloat162float(f[base + i])) - mean;
    q = __builtin_fmaf(d, d, q);
  }
  for (int o = 32; o > 0; o >>= 1) q += __shfl_xor(q, o, 64);
  const float rstd = 1.0f / sqrtf(q / (float)D + 1e-5f);
  for (long long i = threadIdx.x; i < D; i += 64) {
    const float d = __builtin_fmaf(alpha, __bfloat162float(x[base + i]), __bfloat162float(f[base + i])) - mean;
    out[base + i] = __float2bfloat16(__builtin_fmaf(d * rstd, __bfloat162float(g[i]), __bfloat162float(b[i])));
  }
}

extern "C" void kernel_launch(void* const* d_in, const int* in_sizes, int n_in, void* d_out, int out_size, void* d_ws,
                              size_t ws_size, hipStream_t stream) {
  const long long D = in_sizes[2];
  const long long rows = D > 0 ? (long long)in_sizes[0] / D : 0;
  if (rows <= 0) return;
  const uintptr_t align =
      (uintptr_t)d_in[0] | (uintptr_t)d_in[1] | (uintptr_t)d_in[2] | (uintptr_t)d_in[3] | (uintptr_t)d_out;
  if (D == 512 && rows % 16 == 0 && rows * 1024 < (1ll << 31) && (align & 15) == 0) {
    deepnorm512_kernel<<<(unsigned)(rows / 8), 256, 0, stream>>>((const char*)d_in[0], (const char*)d_in[1], 16384ull,
                                                                 (const char*)d_in[2], (const char*)d_in[3],
                                                                 (const char*)d_in[4], (char*)d_out);
  } else {
    deepnorm_generic_kernel<<<(unsigned)rows, 64, 0, stream>>>(
        (const __hip_bfloat16*)d_in[0], (const __hip_bfloat16*)d_in[1], (const __hip_bfloat16*)d_in[2],
        (const __hip_bfloat16*)d_in[3], (const __hip_bfloat16*)d_in[4], (__hip_bfloat16*)d_out, D);
  }
}
```

```cpp
#include <hip/hip_runtime.h>
#include <hip/hip_bf16.h>
#include <stdint.h>

typedef __attribute__((ext_vector_type(4))) unsigned int u32x4;
typedef __attribute__((ext_vector_type(2))) float f32x2;
typedef __attribute__((ext_vector_type(2))) __bf16 bf16x2;

__device__ __forceinline__ f32x2 unpack2(unsigned int u, unsigned int hi_mask) {
  f32x2 r = {__uint_as_float(u << 16), __uint_as_float(u & hi_mask)};
  return r;
}
__device__ __forceinline__ unsigned int pack2(f32x2 v) {
  return __builtin_bit_cast(unsigned int, __builtin_convertvector(v, bf16x2));
}
__device__ __forceinline__ f32x2 fma2(f32x2 a, f32x2 b, f32x2 c) { return __builtin_elementwise_fma(a, b, c); }

template <int CTRL>
__device__ __forceinline__ float dpp_add(float v) {
  int t = __builtin_amdgcn_update_dpp(0, __float_as_int(v), CTRL, 0xf, 0xf, true);
  return v + __int_as_float(t);
}

__device__ __forceinline__ float wave_sum(float v) {
  v = dpp_add<0xB1>(v);
  v = dpp_add<0x4E>(v);
  v = dpp_add<0x141>(v);
  v = dpp_add<0x140>(v);
  v = dpp_add<0x142>(v);
  v = dpp_add<0x143>(v);
  return __int_as_float(__builtin_amdgcn_readlane(__float_as_int(v), 63));
}

__device__ __forceinline__ u32x4 norm_row(const u32x4& xv, const u32x4& fv, const f32x2* G, const f32x2* B, f32x2 al,
                                          unsigned int mask) {
  f32x2 h[4];
#pragma unroll
  for (int j = 0; j < 4; ++j) h[j] = fma2(al, unpack2(xv[j], mask), unpack2(fv[j], mask));
  const f32x2 s = (h[0] + h[1]) + (h[2] + h[3]);
  const float S = wave_sum(s[0] + s[1]);
  const f32x2 SS = {S, S}, nc = {-1.0f / 512.0f, -1.0f / 512.0f};
#pragma unroll
  for (int j = 0; j < 4; ++j) h[j] = fma2(SS, nc, h[j]);
  const f32x2 q = fma2(h[2], h[2], h[0] * h[0]) + fma2(h[3], h[3], h[1] * h[1]);
  f32x2 w[4];
#pragma unroll
  for (int j = 0; j < 4; ++j) w[j] = h[j] * G[j];
  const float r = __builtin_amdgcn_rsqf(__builtin_fmaf(wave_sum(q[0] + q[1]), 1.0f / 512.0f, 1e-5f));
  const f32x2 rr = {r, r};
  u32x4 o;
#pragma unroll
  for (int j = 0; j < 4; ++j) o[j] = pack2(fma2(rr, w[j], B[j]));
  return o;
}

#define NT_LOAD(p) __builtin_nontemporal_load((const u32x4*)(p))

__global__ __attribute__((aligned(2048))) __launch_bounds__(256) void deepnorm512_kernel(
    const char* __restrict__ x, const char* __restrict__ f, const char* __restrict__ g, const char* __restrict__ b,
    const char* __restrict__ alpha_p, char* __restrict__ out) {
  const unsigned int bid = blockIdx.x;
  const unsigned int sbase = (bid >> 1) * 16384u + (bid & 1u) * 4096u + ((bid >> 4) & 1u) * 8192u;
  const unsigned int offA = sbase + threadIdx.x * 16u;
  const unsigned int offB = offA ^ 8192u;
  u32x4 xa = NT_LOAD(x + offA);
  u32x4 fa = NT_LOAD(f + offA);
  __builtin_amdgcn_sched_barrier(0);
  const unsigned int lane16 = (threadIdx.x & 63) * 16u;
  const u32x4 gv = *(const u32x4*)(g + lane16);
  const u32x4 bv = *(const u32x4*)(b + lane16);
  __builtin_amdgcn_sched_barrier(0);
  const unsigned int aw =
      *(const __attribute__((address_space(4))) unsigned int*)((uintptr_t)alpha_p & ~(uintptr_t)3);
  const float alpha = __uint_as_float(((uintptr_t)alpha_p & 2) ? (aw & 0xffff0000u) : (aw << 16));
  const f32x2 al = {alpha, alpha};
  unsigned int mask;
  asm("s_mov_b32 %0, 0xffff0000" : "=s"(mask));
  __amdgpu_buffer_rsrc_t ro = __builtin_amdgcn_make_buffer_rsrc(out, 0, 0xffffffffu, 0x00020000);

  __builtin_amdgcn_sched_barrier(0);
  asm volatile("" : "+v"(xa));
  __builtin_amdgcn_sched_barrier(0);
  const u32x4 xb = NT_LOAD(x + offB);
  __builtin_amdgcn_sched_barrier(0);
  asm volatile("" : "+v"(fa));
  __builtin_amdgcn_sched_barrier(0);
  const u32x4 fb = NT_LOAD(f + offB);
  __builtin_amdgcn_sched_barrier(0);

  f32x2 G[4], B[4];
#pragma unroll
  for (int j = 0; j < 4; ++j) {
    G[j] = unpack2(gv[j], mask);
    B[j] = unpack2(bv[j], mask);
  }
  const u32x4 oa = norm_row(xa, fa, G, B, al, mask);
  __builtin_amdgcn_raw_buffer_store_b128(oa, ro, offA, 0,   16);
  __builtin_amdgcn_sched_barrier(0);
  const u32x4 ob = norm_row(xb, fb, G, B, al, mask);
  __builtin_amdgcn_raw_buffer_store_b128(ob, ro, offB, 0, 0);
}

__global__ __launch_bounds__(64) void deepnorm_generic_kernel(const __hip_bfloat16* __restrict__ x,
                                                              const __hip_bfloat16* __restrict__ f,
                                                              const __hip_bfloat16* __restrict__ g,
                                                              const __hip_bfloat16* __restrict__ b,
                                                              const __hip_bfloat16* __restrict__ alpha_p,
                                                              __hip_bfloat16* __restrict__ out, long long D) {
  const long long base = (long long)blockIdx.x * D;
  const float alpha = __bfloat162float(alpha_p[0]);
  float s = 0.f;
  for (long long i = threadIdx.x; i < D; i += 64)
    s += __builtin_fmaf(alpha, __bfloat162float(x[base + i]), __bfloat162float(f[base + i]));
  for (int o = 32; o > 0; o >>= 1) s += __shfl_xor(s, o, 64);
  const float mean = s / (float)D;
  float q = 0.f;
  for (long long i = threadIdx.x; i < D; i += 64) {
    const float d = __builtin_fmaf(alpha, __bfloat162float(x[base + i]), __bfloat162float(f[base + i])) - mean;
    q = __builtin_fmaf(d, d, q);
  }
  for (int o = 32; o > 0; o >>= 1) q += __shfl_xor(q, o, 64);
  const float rstd = 1.0f / sqrtf(q / (float)D + 1e-5f);
  for (long long i = threadIdx.x; i < D; i += 64) {
    const float d = __builtin_fmaf(alpha, __bfloat162float(x[base + i]), __bfloat162float(f[base + i])) - mean;
    out[base + i] = __float2bfloat16(__builtin_fmaf(d * rstd, __bfloat162float(g[i]), __bfloat162float(b[i])));
  }
}

extern "C" void kernel_launch(void* const* d_in, const int* in_sizes, int n_in, void* d_out, int out_size, void* d_ws,
                              size_t ws_size, hipStream_t stream) {
  const long long D = in_sizes[2];
  const long long rows = D > 0 ? (long long)in_sizes[0] / D : 0;
  if (rows <= 0) return;
  const uintptr_t align =
      (uintptr_t)d_in[0] | (uintptr_t)d_in[1] | (uintptr_t)d_in[2] | (uintptr_t)d_in[3] | (uintptr_t)d_out;
  if (D == 512 && rows % 16 == 0 && rows * 1024 < (1ll << 31) && (align & 15) == 0) {
    deepnorm512_kernel<<<(unsigned)(rows / 8), 256, 0, stream>>>((const char*)d_in[0], (const char*)d_in[1],
                                                                 (const char*)d_in[2], (const char*)d_in[3],
                                                                 (const char*)d_in[4], (char*)d_out);
  } else {
    deepnorm_generic_kernel<<<(unsigned)rows, 64, 0, stream>>>(
        (const __hip_bfloat16*)d_in[0], (const __hip_bfloat16*)d_in[1], (const __hip_bfloat16*)d_in[2],
        (const __hip_bfloat16*)d_in[3], (const __hip_bfloat16*)d_in[4], (__hip_bfloat16*)d_out, D);
  }
}
```
